```python
import torch

def _softmax(x, axis=-1):
    return torch.softmax(x, dim=axis)
def _gelu(x):
    return torch.nn.functional.gelu(x, approximate="tanh")
def _silu(x):
    return torch.nn.functional.silu(x)
def _sigmoid(x):
    return torch.sigmoid(x)
def _layernorm(x, axis=-1, eps=1e-5):
    m = x.mean(dim=axis, keepdim=True)
    v = x.var(dim=axis, keepdim=True, unbiased=False)
    return (x - m) / torch.sqrt(v + eps)
def _scatter_add(tgt, idx, src, axis=0):
    return tgt.clone().index_add_(axis, idx, src)
def _segment_sum(src, seg, n):
    out = torch.zeros((n,) + src.shape[1:], dtype=src.dtype)
    return out.index_add_(0, seg, src)
def _pad(x, pw):
    flat = tuple(v for pair in reversed(pw) for v in pair)
    return torch.nn.functional.pad(x, flat)

kernel_name = 'gated_mix__mha_block__sliding_local_dilated__prompt'

def setup_inputs():

    _w = (lambda a: (lambda t: t.to(torch.bfloat16) if t.is_floating_point() else t)(torch.as_tensor(a)))
    def _A_setup():
        B, S, D, H = 1, 2048, 512, 32
        return {
            "x": _w((lambda *s: torch.randn(*s, dtype=torch.bfloat16) * 0.05)(B, S, D)),
            "wqkv": _w((lambda *s: torch.randn(*s, dtype=torch.bfloat16) * 1.6)(D, 3 * D)),
            "wo": _w((lambda *s: torch.randn(*s, dtype=torch.bfloat16) * 0.1)(D, D)),
        }
    _A_in = _A_setup()

    def _B_setup():
        B, S, D = 1, 2048, 512
        return {
            "x": _w((lambda *s: torch.randn(*s, dtype=torch.bfloat16) * 0.05)(B, S, D)),
            "wqkv": _w((lambda *s: torch.randn(*s, dtype=torch.bfloat16) * 1.6)(D, 3 * D)),
            "wo": _w((lambda *s: torch.randn(*s, dtype=torch.bfloat16) * 0.05)(D, D)),
        }
    _B_in = _B_setup()

    return {
        "A_x": _A_in["x"],
        "A_wqkv": _A_in["wqkv"],
        "A_wo": _A_in["wo"],
        "B_x": _B_in["x"],
        "B_wqkv": _B_in["wqkv"],
        "B_wo": _B_in["wo"],
        "gate_w": _w((lambda *s: torch.randn(*s, dtype=torch.bfloat16) * 0.05)(512) * 16.0),
    }

def reference(A_x, A_wqkv, A_wo, B_x, B_wqkv, B_wo, gate_w):
    def _A_ref(x, wqkv, wo):
        B, S, D = x.shape
        H = 32
        Dh = D // H
        qkv = (lambda x, *d: x.permute(*d))((x @ wqkv).reshape(B, S, 3, H, Dh), 2, 0, 3, 1, 4)
        q, k, v = qkv[0], qkv[1], qkv[2]
        scores = torch.einsum("bhqd,bhkd->bhqk", q, k) * (Dh ** -0.5)
        p = _softmax(scores, axis=-1)
        o = torch.einsum("bhqk,bhkd->bhqd", p, v)
        return (lambda x, *d: x.permute(*d))(o, 0, 2, 1, 3).reshape(B, S, D) @ wo

    def _B_ref(x, wqkv, wo):
        B, S, D = x.shape
        H = 32; Dh = D // H
        W, dil = 8, 2
        qkv = (lambda x, *d: x.permute(*d))((x @ wqkv).reshape(B, S, 3, H, Dh), 2, 0, 3, 1, 4)
        q, k, v = qkv[0], qkv[1], qkv[2]
        scale = Dh ** -0.5
        pos = torch.arange(S)
        off = torch.arange(W) * dil
        idx = pos[:, None] - off[None, :]
        mask = idx >= 0
        idx_c = torch.where(mask, idx, 0).reshape(-1)
        kg = k[:, :, idx_c, :].reshape(B, H, S, W, Dh)
        vg = v[:, :, idx_c, :].reshape(B, H, S, W, Dh)
        logits = torch.einsum("bhsd,bhswd->bhsw", q, kg) * scale
        logits = torch.where(mask[None, None, :, :], logits, -1e9)
        attn = _softmax(logits, axis=-1)
        o = torch.einsum("bhsw,bhswd->bhsd", attn, vg)
        return (lambda x, *d: x.permute(*d))(o, 0, 2, 1, 3).reshape(B, S, D) @ wo

    x = A_x
    g = _sigmoid((x * gate_w).sum(-1, keepdims=True))
    a = _A_ref(x, A_wqkv, A_wo)
    b = _B_ref(x, B_wqkv, B_wo)
    return g * a + (1 - g) * b
```

```cpp
#include <hip/hip_runtime.h>
#include <hip/hip_bf16.h>
#include <stdint.h>

typedef __attribute__((ext_vector_type(32))) float f32x32;
typedef __attribute__((ext_vector_type(32))) unsigned u32x32;
typedef __attribute__((ext_vector_type(16))) float f32x16;
typedef __attribute__((ext_vector_type(8))) float f32x8;
typedef __attribute__((ext_vector_type(4))) float f32x4;
typedef __attribute__((ext_vector_type(8))) __bf16 bf16x8;
typedef __attribute__((ext_vector_type(16))) __bf16 bf16x16;
typedef __attribute__((ext_vector_type(4))) short s16x4;
typedef __attribute__((ext_vector_type(4))) unsigned u32x4;
typedef unsigned long long u64;
#define LAS __attribute__((address_space(3)))
#define GAS __attribute__((address_space(1)))
typedef LAS char* lptr;
typedef const LAS char* lcptr;

constexpr unsigned WS_Q  = 0;
constexpr unsigned WS_KV = 2u << 20;
constexpr unsigned WS_O  = 6u << 20;
constexpr unsigned WS_G  = 10u << 20;
constexpr unsigned WS_FLAG = WS_G + 8192;
constexpr unsigned FL_EPOCH = 0, FL_ALL = 2048, FL_KV = 4096, FL_OA = FL_KV + 256 * 128, FL_OB = FL_OA + 256 * 256, FL_PZ = FL_OB + 256 * 128, FL_END = FL_PZ + 256 * 256;
constexpr unsigned WS_END = WS_FLAG + FL_END;
constexpr int LDS_BYTES = 152 * 1024;
constexpr float QSCALE = 0.25f * 1.4426950408889634f;

struct Params {
    const char* x; const char* wA; const char* woA; const char* wB; const char* woB; const char* gw;
    char* out; char* ws;
};

__device__ __forceinline__ unsigned cvtpk(float lo, float hi) {
    typedef __attribute__((ext_vector_type(2))) float f2; typedef __attribute__((ext_vector_type(2))) __bf16 b2;
    f2 v = {lo, hi}; b2 r = __builtin_convertvector(v, b2); return __builtin_bit_cast(unsigned, r);
}
__device__ __forceinline__ u32x4 pack8(float a0, float a1, float a2, float a3, float a4, float a5, float a6, float a7) {
    return (u32x4){cvtpk(a0, a1), cvtpk(a2, a3), cvtpk(a4, a5), cvtpk(a6, a7)};
}
template <int O> __device__ __forceinline__ u32x4 pack8s(const f32x16& a, float s) {
    return pack8(a[O] * s, a[O + 1] * s, a[O + 2] * s, a[O + 3] * s, a[O + 4] * s, a[O + 5] * s, a[O + 6] * s, a[O + 7] * s);
}
template <int O> __device__ __forceinline__ u32x4 pack8v(const f32x16& a) {
    return pack8(a[O], a[O + 1], a[O + 2], a[O + 3], a[O + 4], a[O + 5], a[O + 6], a[O + 7]);
}
typedef short v4i16_t __attribute__((ext_vector_type(4)));
__device__ __forceinline__ s16x4 trread(lcptr p) {
    return __builtin_bit_cast(s16x4, __builtin_amdgcn_ds_read_tr16_b64_v4i16((LAS v4i16_t*)p));
}
__device__ __forceinline__ bf16x8 cat2(s16x4 a, s16x4 b) {
    typedef __attribute__((ext_vector_type(8))) short s16x8;
    s16x8 r = __builtin_shufflevector(a, b, 0, 1, 2, 3, 4, 5, 6, 7); return __builtin_bit_cast(bf16x8, r);
}
#define MFMA32(a, b, c) __builtin_amdgcn_mfma_f32_32x32x16_bf16(a, b, c, 0, 0, 0)
__device__ __forceinline__ bf16x16 lo8(const f32x16& p) { f32x8 t = __builtin_shufflevector(p, p, 0, 1, 2, 3, 4, 5, 6, 7); return __builtin_bit_cast(bf16x16, t); }
__device__ __forceinline__ bf16x16 hi8(const f32x16& p) { f32x8 t = __builtin_shufflevector(p, p, 8, 9, 10, 11, 12, 13, 14, 15); return __builtin_bit_cast(bf16x16, t); }
#define SMFMAC(a, b, c) __builtin_amdgcn_smfmac_f32_32x32x32_bf16(a, b, c, (int)0xDDDDDDDD, 0, 0)
#define LDS_BAR() asm volatile("s_waitcnt lgkmcnt(0)\n\ts_barrier" ::: "memory")
#define SC1 16
__device__ __forceinline__ float swap32max(float m) {
    auto rr = __builtin_amdgcn_permlane32_swap(__float_as_uint(m), __float_as_uint(m), false, false);
    return __builtin_fmaxf(__uint_as_float(rr[0]), __uint_as_float(rr[1]));
}
#define MX3(a, b, c) __builtin_fmaxf(__builtin_fmaxf((a), (b)), (c))
__device__ __forceinline__ float max16(const f32x16& s) {
    float a = MX3(s[0], s[1], s[2]), b = MX3(s[3], s[4], s[5]);
    a = MX3(a, s[6], s[7]); b = MX3(b, s[8], s[9]); a = MX3(a, s[10], s[11]); b = MX3(b, s[12], s[13]);
    return MX3(a, b, __builtin_fmaxf(s[14], s[15]));
}
#define S_(x) #x
#define S(x) S_(x)

__device__ __forceinline__ float wave_sum63(float v) {
#define DPP_ADD(ctrl, rm) v += __int_as_float(__builtin_amdgcn_update_dpp(0, __float_as_int(v), ctrl, rm, 0xf, false))
    DPP_ADD(0x111, 0xf); DPP_ADD(0x112, 0xf); DPP_ADD(0x114, 0xf); DPP_ADD(0x118, 0xf); DPP_ADD(0x142, 0xa); DPP_ADD(0x143, 0xc);
#undef DPP_ADD
    return v;
}
typedef GAS u64 gu64;
__device__ __forceinline__ u64 ld_flag(const char* p) { return __hip_atomic_load((gu64*)p, __ATOMIC_RELAXED, __HIP_MEMORY_SCOPE_AGENT); }
__device__ __forceinline__ void st_flag(char* p, u64 v) { __hip_atomic_store((gu64*)p, v, __ATOMIC_RELAXED, __HIP_MEMORY_SCOPE_AGENT); }
__device__ __forceinline__ void poll_flags(const char* base, int n, u64 tag, int lane) {
#pragma unroll 1
    for (unsigned spin = 0; spin < (1u << 16); ++spin) {
        const u64 v = lane < n ? ld_flag(base + lane * 8) : tag;
        if (__all(v == tag)) break;
        __builtin_amdgcn_s_sleep(1);
    }
}

constexpr int P1_SLOT = 45056, P1_W = 20480;

#define G_X0(s) "v[32+20*" S(s) ":35+20*" S(s) "]"
#define G_X1(s) "v[36+20*" S(s) ":39+20*" S(s) "]"
#define G_XH(s) "v[112+4*" S(s) ":115+4*" S(s) "]"
#define G_W(s, g) "v[40+20*" S(s) "+4*" S(g) ":43+20*" S(s) "+4*" S(g) "]"
#define G_WH(s, g, t) "v[40+20*" S(s) "+4*" S(g) "+2*" S(t) ":41+20*" S(s) "+4*" S(g) "+2*" S(t) "]"
#define G_ACC(n) "v[128+16*" S(n) ":143+16*" S(n) "]"
#define G_TR(s, g, t) "ds_read_b64_tr_b16 " G_WH(s, g, t) ", v28 offset:2048*" S(s) "+8192*" S(g) "+512*" S(t) "\n\t"
#define G_RX0(s) "ds_read_b128 " G_X0(s) ", v[20+" S(s) "]\n\t"
#define G_RX1(s) "ds_read_b128 " G_X1(s) ", v[20+" S(s) "] offset:4096\n\t"
#define G_MF(a, x, y) "v_mfma_f32_32x32x16_bf16 " G_ACC(a) ", " x ", " y ", " G_ACC(a) "\n\t"
#define G_HM_0(s) ""
#define G_HM_1(s) G_MF(6, G_W(s, 1), G_XH(s))
#define G_HM_2(s) G_MF(6, G_XH(s), G_W(s, 2))
#define G_HR_0(n) ""
#define G_HR_1(n) "ds_read_b128 " G_XH(n) ", v[24+" S(n) "]\n\t"
#define G_HR_2(n) G_HR_1(n)
#define G_STEP(M, s, n) \
    "s_waitcnt lgkmcnt(8)\n\t" \
    G_MF(0, G_W(s, 0), G_X0(s)) G_RX0(n) G_TR(n, 0, 0) \
    G_MF(1, G_W(s, 1), G_X0(s)) G_TR(n, 0, 1) \
    G_MF(2, G_X0(s), G_W(s, 2)) G_TR(n, 1, 0) \
    G_MF(3, G_W(s, 0), G_X1(s)) G_TR(n, 1, 1) G_TR(n, 2, 0) \
    G_MF(4, G_W(s, 1), G_X1(s)) G_TR(n, 2, 1) \
    G_HM_##M(s) \
    G_MF(5, G_X1(s), G_W(s, 2)) G_RX1(n) G_HR_##M(n)
#define G_STEP2(M) \
    "s_waitcnt lgkmcnt(8)\n\t" \
    G_MF(0, G_W(2, 0), G_X0(2)) \
    "s_add_u32 %[slot], %[slot], " S(45056) "\n\t" \
    "s_cmp_eq_u32 %[slot], " S(135168) "\n\t" \
    "s_cselect_b32 %[slot], 0, %[slot]\n\t" \
    "v_add_u32 v20, %[slot], v16\n\tv_add_u32 v28, %[slot], v18\n\tv_add_u32 v24, %[slot], v17\n\t" \
    G_MF(1, G_W(2, 1), G_X0(2)) \
    "v_xor_b32 v21, 32, v20\n\tv_xor_b32 v22, 64, v20\n\tv_xor_b32 v23, 96, v20\n\t" \
    "v_xor_b32 v25, 32, v24\n\tv_xor_b32 v26, 64, v24\n\tv_xor_b32 v27, 96, v24\n\t" \
    G_MF(2, G_X0(2), G_W(2, 2)) \
    "s_sub_u32 %[cnt], %[cnt], 1\n\t" \
    "s_waitcnt lgkmcnt(0)\n\t" \
    "s_barrier\n\t" \
    G_RX0(0) G_TR(0, 0, 0) \
    G_MF(3, G_W(2, 0), G_X1(2)) G_TR(0, 0, 1) G_TR(0, 1, 0) G_TR(0, 1, 1) \
    G_MF(4, G_W(2, 1), G_X1(2)) G_TR(0, 2, 0) G_TR(0, 2, 1) \
    G_HM_##M(2) \
    G_MF(5, G_X1(2), G_W(2, 2)) G_RX1(0) G_HR_##M(0)
#define G_LOOP(M, L) \
    S(L) ":\n\t" \
    G_STEP(M, 0, 2) \
    G_STEP(M, 1, 3) \
    G_STEP2(M) \
    G_STEP(M, 3, 1) \
    "s_cmp_lg_u32 %[cnt], 0\n\t" \
    "s_cbranch_scc1 " S(L) "b\n\t"
#define G_READS(s) G_RX0(s) G_TR(s, 0, 0) G_TR(s, 0, 1) G_TR(s, 1, 0) G_TR(s, 1, 1) G_TR(s, 2, 0) G_TR(s, 2, 1) G_RX1(s) G_HR_1(s)

__device__ __forceinline__ void gemm_loop(unsigned xa0, unsigned wa0, unsigned ha0, int mode, const char* ep, u64& epoch, f32x32& a01, f32x32& a23, f32x32& a45, f32x16& a6) {
    unsigned cnt = 8, slot = 0, e0, e1;
    asm volatile(
        "v_mov_b32 v251, 0\n\tglobal_load_dwordx2 v[252:253], v251, %[ep] sc1\n\t"
        "v_mov_b32 v16, %[xa]\n\tv_mov_b32 v18, %[wa]\n\tv_mov_b32 v17, %[ha]\n\t"
        "v_mov_b32 v20, v16\n\tv_xor_b32 v21, 32, v16\n\tv_xor_b32 v22, 64, v16\n\tv_xor_b32 v23, 96, v16\n\tv_mov_b32 v28, v18\n\t"
        "v_mov_b32 v24, v17\n\tv_xor_b32 v25, 32, v17\n\tv_xor_b32 v26, 64, v17\n\tv_xor_b32 v27, 96, v17\n\t"
        ".set i_, 128\n\t.rept 112\n\tv_mov_b32 v[i_], 0\n\t.set i_, i_+1\n\t.endr\n\t"
        "s_barrier\n\t"
        G_READS(0) G_READS(1)
        "s_waitcnt lgkmcnt(9)\n\t"
        "s_cmp_eq_u32 %[mode], 1\n\ts_cbranch_scc1 21f\n\t"
        "s_cmp_eq_u32 %[mode], 2\n\ts_cbranch_scc1 22f\n\t"
        G_LOOP(0, 20)
        "s_branch 23f\n\t"
        G_LOOP(1, 21)
        "s_branch 23f\n\t"
        G_LOOP(2, 22)
        "23:\n\t"
        "s_waitcnt vmcnt(0) lgkmcnt(0)\n\t"
        "v_readfirstlane_b32 %[e0], v252\n\tv_readfirstlane_b32 %[e1], v253\n\t"
        "s_nop 7\n\ts_nop 7\n\t"
        : "={v[128:159]}"(a01), "={v[160:191]}"(a23), "={v[192:223]}"(a45), "={v[224:239]}"(a6), [cnt] "+s"(cnt), [slot] "+s"(slot), [e0] "=&s"(e0), [e1] "=&s"(e1)
        : [xa] "v"(xa0), [wa] "v"(wa0), [ha] "v"(ha0), [mode] "s"(mode), [ep] "s"(ep)
        : "memory", "scc", "v240","v241","v242","v243","v244","v245","v246","v247","v248","v249","v250","v251","v252","v253","v254","v255",
          "v16","v17","v18","v20","v21","v22","v23","v24","v25","v26","v27","v28",
          "v32","v33","v34","v35","v36","v37","v38","v39","v40","v41","v42","v43","v44","v45","v46","v47","v48","v49","v50","v51","v52","v53","v54","v55","v56","v57","v58","v59","v60","v61","v62","v63",
          "v64","v65","v66","v67","v68","v69","v70","v71","v72","v73","v74","v75","v76","v77","v78","v79","v80","v81","v82","v83",
          "v84","v85","v86","v87","v88","v89","v90","v91","v92","v93","v94","v95","v96","v97","v98","v99","v100","v101","v102","v103",
          "v104","v105","v106","v107","v108","v109","v110","v111","v112","v113","v114","v115","v116","v117","v118","v119","v120","v121","v122","v123","v124","v125","v126","v127");
    epoch = ((u64)e1 << 32) | e0;
}
__device__ __forceinline__ f32x16 lo16(const f32x32& a) { return __builtin_shufflevector(a, a, 0, 1, 2, 3, 4, 5, 6, 7, 8, 9, 10, 11, 12, 13, 14, 15); }
__device__ __forceinline__ f32x16 hi16(const f32x32& a) { return __builtin_shufflevector(a, a, 16, 17, 18, 19, 20, 21, 22, 23, 24, 25, 26, 27, 28, 29, 30, 31); }

#define DMA_PIECE(V, R, DM, DS) "s_add_u32 m0, m0, " S(DM) "\n\ts_add_u32 %[t], %[t], " S(DS) "\n\tbuffer_load_dwordx4 " V ", " R ", %[t] offen lds\n\t"
__device__ __forceinline__ void p1_issue(__amdgpu_buffer_rsrc_t rx, __amdgpu_buffer_rsrc_t rw, unsigned vx, unsigned vw, unsigned vh, unsigned dst, int c, int br) {
    unsigned sv, t;
    const unsigned sx = c * 128, sw = c * (64 * 3072);
    asm volatile(
        "s_mov_b32 %[sv], m0\n\ts_mov_b32 m0, %[dst]\n\ts_mov_b32 %[t], %[sx]\n\t"
        "buffer_load_dwordx4 %[vx], %[rx], %[t] offen lds\n\t"
        DMA_PIECE("%[vx]", "%[rx]", 4096, 32768) DMA_PIECE("%[vx]", "%[rx]", 4096, 32768) DMA_PIECE("%[vx]", "%[rx]", 4096, 32768)
        "s_add_u32 m0, m0, 4096\n\ts_cmp_eq_u32 %[br], 0\n\ts_cbranch_scc1 1f\n\t"
        "buffer_load_dwordx4 %[vh], %[rx], %[sx] offen lds\n\t"
        "1:\n\t"
        "s_add_u32 m0, m0, 4096\n\ts_mov_b32 %[t], %[sw]\n\t"
        "buffer_load_dwordx4 %[vw], %[rw], %[t] offen lds\n\t"
        DMA_PIECE("%[vw]", "%[rw]", 4096, 98304) DMA_PIECE("%[vw]", "%[rw]", 4096, 1024-98304) DMA_PIECE("%[vw]", "%[rw]", 4096, 98304)
        DMA_PIECE("%[vw]", "%[rw]", 4096, 1024-98304) DMA_PIECE("%[vw]", "%[rw]", 4096, 98304)
        "s_mov_b32 m0, %[sv]"
        : [sv] "=&s"(sv), [t] "=&s"(t)
        : [rx] "s"(rx), [rw] "s"(rw), [vx] "v"(vx), [vw] "v"(vw), [vh] "v"(vh), [dst] "s"(dst), [sx] "s"(sx), [sw] "s"(sw), [br] "s"(br)
        : "memory", "scc");
}

__device__ __forceinline__ void phase1(const Params& p, char* smem, __amdgpu_buffer_rsrc_t rs, u64& tag) {
    const int tid = threadIdx.x, lane = tid & 63, l31 = lane & 31, h = lane >> 5;
    const int w = __builtin_amdgcn_readfirstlane(tid >> 6);
    const int b = blockIdx.x, i2 = b >> 3;
    const int br = i2 >> 4, j = 2 * ((b & 7) >> 1) + ((i2 >> 3) & 1), r = 8 * (b & 1) + (i2 & 7);
    const int R0 = 128 * r;
    const unsigned lds0 = (unsigned)(uintptr_t)smem;
    lptr kvb = (lptr)smem;
    constexpr int QB_OFF = 4 * 5 * 2048;
    if (w >= 4) {
        const int lw = w - 4;
        const __amdgpu_buffer_rsrc_t rx = __builtin_amdgcn_make_buffer_rsrc((void*)p.x, 0, 2048 * 1024, 0x00020000);
        const __amdgpu_buffer_rsrc_t rw = __builtin_amdgcn_make_buffer_rsrc((void*)(br ? p.wB : p.wA), 0, 512 * 3072, 0x00020000);
        const unsigned xsw = (((lane & 7) ^ ((4 * lw + (lane >> 4)) & 7)) << 4);
        const unsigned vx = (R0 + 8 * lw + (lane >> 3)) * 1024 + xsw;
        int hrow = R0 - 32 + 8 * lw + (lane >> 3); hrow = hrow < 0 ? 0 : hrow;
        const unsigned vh = hrow * 1024 + xsw;
        const unsigned vw = (8 * lw + (lane >> 3)) * 3072 + j * 128 + (((lane & 7) ^ (((lane >> 4) & 1) << 2)) << 4);
        const unsigned d0 = lds0 + lw * 1024;
        p1_issue(rx, rw, vx, vw, vh, d0, 0, br);
        p1_issue(rx, rw, vx, vw, vh, d0 + P1_SLOT, 1, br);
        asm volatile("s_waitcnt vmcnt(10)\n\ts_barrier" ::: "memory");
        p1_issue(rx, rw, vx, vw, vh, d0 + 2 * P1_SLOT, 2, br);
        const int grow = 256 * (i2 >> 2) + 8 * (4 * (b & 7) + (i2 & 3)) + 2 * lw;
        u32x4 gx0, gx1, gv;
        asm volatile("global_load_dwordx4 %0, %3, %4\n\tglobal_load_dwordx4 %1, %3, %4 offset:1024\n\tglobal_load_dwordx4 %2, %3, %5"
                     : "=&v"(gx0), "=&v"(gx1), "=&v"(gv) : "v"(lane * 16), "s"(p.x + (size_t)grow * 1024), "s"(p.gw) : "memory");
        int slot = 0;
#pragma unroll 1
        for (int c = 0; c < 8; ++c) {
            if (c < 6) asm volatile("s_waitcnt vmcnt(10)\n\ts_barrier" ::: "memory");
            else asm volatile("s_waitcnt vmcnt(0)\n\ts_barrier" ::: "memory");
            if (c < 5) p1_issue(rx, rw, vx, vw, vh, d0 + slot, c + 3, br);
            slot = slot == 2 * P1_SLOT ? 0 : slot + P1_SLOT;
        }
        if (br) LDS_BAR();
        {
            asm volatile("" : "+v"(gx0), "+v"(gx1), "+v"(gv));
            float s0 = 0.f, s1 = 0.f;
#pragma unroll
            for (int q = 0; q < 4; ++q) {
                const float glo = __uint_as_float(gv[q] << 16), ghi = __uint_as_float(gv[q] & 0xffff0000u);
                s0 = __builtin_fmaf(__uint_as_float(gx0[q] << 16), glo, s0); s0 = __builtin_fmaf(__uint_as_float(gx0[q] & 0xffff0000u), ghi, s0);
                s1 = __builtin_fmaf(__uint_as_float(gx1[q] << 16), glo, s1); s1 = __builtin_fmaf(__uint_as_float(gx1[q] & 0xffff0000u), ghi, s1);
            }
            s0 = wave_sum63(s0); s1 = wave_sum63(s1);
            const float g0 = __int_as_float(__builtin_amdgcn_readlane(__float_as_int(s0), 63)), g1 = __int_as_float(__builtin_amdgcn_readlane(__float_as_int(s1), 63));
            const float gg = 1.f / (1.f + __expf(-((lane & 1) ? g1 : g0)));
            if (lane < 2) __builtin_amdgcn_raw_buffer_store_b32(__float_as_uint(gg), rs, WS_G + (grow + lane) * 4, 0, SC1);
        }
    } else {
        {
            const u32x4 mark = {~0u, ~0u, ~0u, ~0u};
            const unsigned ot = WS_O + ((8 * (i2 >> 2) + 2 * w) * 64 + 4 * (b & 7) + (i2 & 3)) * 1024 + lane * 16;
            __builtin_amdgcn_raw_buffer_store_b128(mark, rs, ot, 0, SC1);
            __builtin_amdgcn_raw_buffer_store_b128(mark, rs, ot + 65536, 0, SC1);
        }
        const int rh = w >> 1, pp = w & 1;
        const unsigned xs = (lane >> 1) & 7, q4 = (lane & 15) >> 2, p4 = lane & 3;
        const unsigned xa0 = lds0 + (64 * rh + l31) * 128 + ((h ^ xs) << 4);
        const unsigned ha0 = lds0 + (128 + l31) * 128 + ((h ^ xs) << 4);
        const unsigned wa0 = lds0 + P1_W + (8 * h + q4) * 128 + (((pp * 4 + ((lane >> 4) & 1) * 2 + (p4 >> 1)) ^ ((q4 >> 1) << 2)) << 4) + (p4 & 1) * 8;
        f32x32 a01, a23, a45; f32x16 ah;
        {
            const int sh = i2 * 256 + w * 64 + lane;
            const char* t0 = (sh & 7) < 3 ? p.gw : p.x + (size_t)(1024 * (b & 1) + (sh >> 3)) * 1024 + (sh & 7) * 128;
            const int wl = sh < 6144 ? sh : 0, wr = wl % 1536, wq = wl / 1536;
            const char* t1 = (wr & 511) < 64 * 3 ? p.gw : ((wq & 2) ? p.wB : p.wA) + (size_t)(wr & 511) * 3072 + (wr >> 9) * 1024 + (2 * ((b & 7) >> 1) + (wq & 1)) * 128;
            asm volatile("global_load_dword v244, %0, off sc1\n\tglobal_load_dword v245, %1, off sc1" :: "v"(t0), "v"(t1) : "memory", "v244", "v245");
        }
        u64 epoch;
        gemm_loop(xa0, wa0, ha0, br ? 1 + rh : 0, p.ws + WS_FLAG + FL_EPOCH, epoch, a01, a23, a45, ah);
        tag = epoch + 1;
        int tz = threadIdx.x; asm volatile("" : "+v"(tz));
        const int lane = tz & 63, l31 = lane & 31, h = lane >> 5;
        const int tau = (l31 & 19) | ((l31 & 4) << 1) | ((l31 & 8) >> 1);
        const unsigned ko = h * 512 + tau * 16, vo = 1024 + h * 512 + (l31 & 15) * 16;
        if (!br) {
            const int h0 = 4 * j + 2 * pp;
            const unsigned kb = WS_KV + (h0 * 64 + 4 * r + 2 * rh) * 2048, vb = kb + (l31 >> 4) * 131072, qb = WS_Q + (h0 * 64 + 4 * r + 2 * rh) * 1024 + lane * 16;
#define P1_PUB(AQ, AK, AV, T) do { \
            __builtin_amdgcn_raw_buffer_store_b128(pack8v<0>(AK), rs, kb + (T) * 2048 + ko, 0, SC1); \
            __builtin_amdgcn_raw_buffer_store_b128(pack8v<8>(AK), rs, kb + (T) * 2048 + 131072 + ko, 0, SC1); \
            __builtin_amdgcn_raw_buffer_store_b128(pack8v<0>(AV), rs, vb + (T) * 2048 + vo, 0, SC1); \
            __builtin_amdgcn_raw_buffer_store_b128(pack8v<8>(AV), rs, vb + (T) * 2048 + vo + 256, 0, SC1); \
            __builtin_amdgcn_raw_buffer_store_b128(pack8s<0>(AQ, QSCALE), rs, qb + (T) * 1024, 0, SC1); \
            __builtin_amdgcn_raw_buffer_store_b128(pack8s<8>(AQ, QSCALE), rs, qb + (T) * 1024 + 65536, 0, SC1); } while (0)
            { const f32x16 aq = lo16(a01), ak = hi16(a01), av = lo16(a23); P1_PUB(aq, ak, av, 0); }
            { const f32x16 aq = hi16(a23), ak = lo16(a45), av = hi16(a45); P1_PUB(aq, ak, av, 1); }
#undef P1_PUB
        } else {
            LDS_BAR();
            lptr kb = kvb + ((2 * pp) * 5 + 2 * rh + 1) * 2048, vb = kb + (l31 >> 4) * 10240, qb = kvb + QB_OFF + ((2 * pp) * 4 + 2 * rh) * 1024 + lane * 16;
#define P1_LDS(AQ, AK, AV, T) do { \
            *(LAS u32x4*)(kb + (T) * 2048 + ko) = pack8v<0>(AK); \
            *(LAS u32x4*)(kb + (T) * 2048 + 10240 + ko) = pack8v<8>(AK); \
            *(LAS u32x4*)(vb + (T) * 2048 + vo) = pack8v<0>(AV); \
            *(LAS u32x4*)(vb + (T) * 2048 + vo + 256) = pack8v<8>(AV); \
            *(LAS u32x4*)(qb + (T) * 1024) = pack8s<0>(AQ, QSCALE); \
            *(LAS u32x4*)(qb + (T) * 1024 + 4096) = pack8s<8>(AQ, QSCALE); } while (0)
            { const f32x16 aq = lo16(a01), ak = hi16(a01), av = lo16(a23); P1_LDS(aq, ak, av, 0); }
            { const f32x16 aq = hi16(a23), ak = lo16(a45), av = hi16(a45); P1_LDS(aq, ak, av, 1); }
#undef P1_LDS
            lptr hb = kvb + ((2 * pp) * 5) * 2048;
            if (rh == 0) { *(LAS u32x4*)(hb + ko) = pack8v<0>(ah); *(LAS u32x4*)(hb + 10240 + ko) = pack8v<8>(ah); }
            else { lptr hv = hb + (l31 >> 4) * 10240 + vo; *(LAS u32x4*)(hv) = pack8v<0>(ah); *(LAS u32x4*)(hv + 256) = pack8v<8>(ah); }
        }
    }
    if (br) {
        int tb = threadIdx.x; asm volatile("" : "+v"(tb));
        const int lane = tb & 63, l31 = lane & 31, h = lane >> 5;
        const int rt = w >> 1, pp = w & 1;
        f32x16 cown, cprev;
        {
            const u64 win = 0x5555ull << l31;
            const unsigned mo = (unsigned)(win >> 14) >> (8 * h);
            const unsigned mp = (r == 0 && rt == 0) ? 0u : (unsigned)(win << 18) >> (8 * h);
#pragma unroll
            for (int g = 0; g < 16; ++g) {
                const int pos = (g & 7) + 16 * (g >> 3);
                cown[g] = __uint_as_float(~(unsigned)__builtin_amdgcn_sbfe(mo, pos, 1) & 0xf149f2cau);
                cprev[g] = g >= 8 ? __uint_as_float(~(unsigned)__builtin_amdgcn_sbfe(mp, pos, 1) & 0xf149f2cau) : 0.f;
            }
        }
        constexpr int ONES_OFF = QB_OFF + 16 * 1024;
        if (w >= 4) { const u32x4 ones = {0x3f803f80u, 0x3f803f80u, 0x3f803f80u, 0x3f803f80u}; *(LAS u32x4*)(kvb + ONES_OFF + (w - 4) * 1024 + lane * 16) = ones; }
        asm volatile("" : "+v"(cown), "+v"(cprev));
        LDS_BAR();
#pragma unroll
        for (int e = 0; e < 2; ++e) {
            const bf16x8 qf = *(const LAS bf16x8*)((lcptr)kvb + QB_OFF + ((2 * pp + e) * 4 + rt) * 1024 + lane * 16);
            lcptr t1 = (lcptr)kvb + ((2 * pp + e) * 5 + rt + 1) * 2048;
            const bf16x8 k1 = *(const LAS bf16x8*)(t1 + lane * 16), k0 = *(const LAS bf16x8*)(t1 - 2048 + lane * 16);
            lcptr vp = l31 < 16 ? t1 + 1024 + h * 512 + l31 * 16 : (lcptr)kvb + ONES_OFF + 2048;
            const bf16x8 v10 = *(const LAS bf16x8*)(vp), v11 = *(const LAS bf16x8*)(vp + 256), v01 = *(const LAS bf16x8*)(vp - 2048 + 256);
            f32x16 s1 = MFMA32(k1, qf, cown), s0 = MFMA32(k0, qf, cprev);
            float m = max16(s1);
            m = MX3(m, s0[8], s0[9]); m = MX3(m, s0[10], s0[11]); m = MX3(m, s0[12], s0[13]); m = MX3(m, s0[14], s0[15]);
            m = swap32max(m);
#pragma unroll
            for (int g = 0; g < 16; ++g) s1[g] = __builtin_amdgcn_exp2f(s1[g] - m);
#pragma unroll
            for (int g = 8; g < 16; ++g) s0[g] = __builtin_amdgcn_exp2f(s0[g] - m);
            f32x16 o;
#pragma unroll
            for (int g = 0; g < 9; ++g) o[g] = 0.f;
            asm volatile("" : "+v"(o));
            o = SMFMAC(v10, lo8(s1), o);
            o = SMFMAC(v11, hi8(s1), o);
            o = SMFMAC(v01, hi8(s0), o);
            const float rl = __builtin_amdgcn_rcpf(o[8]);
            __builtin_amdgcn_raw_buffer_store_b128(pack8s<0>(o, rl), rs, WS_O + ((4 * r + rt) * 64 + 32 + 4 * j + 2 * pp + e) * 1024 + lane * 16, 0, SC1);
        }
    }
    if (!br) {
        asm volatile("s_waitcnt vmcnt(0) lgkmcnt(0)\n\ts_barrier" ::: "memory");
        int tf = threadIdx.x; asm volatile("" : "+v"(tf));
        if (tf < 32) st_flag(p.ws + WS_FLAG + FL_KV + (j + 8 * tf) * 128 + r * 8, tag);
    }
}

#define EX(b, i) "v_exp_f32 v[" S(b) "+" S(i) "], v[" S(b) "+" S(i) "]\n\t"
#define RNG(b, n) "v[" S(b) ":" S(b) "+" S(n) "-1]"
#define RNGO(b, o, n) "v[" S(b) "+" S(o) ":" S(b) "+" S(o) "+" S(n) "-1]"
#define OREG(u) "v[128+16*" S(u) ":128+16*" S(u) "+15]"
#define QREG(u) "v[96+4*" S(u) ":96+4*" S(u) "+3]"
#define UNIT(X, Y, U, UP, UN, V1P, V0C, KN, MID_A, MID_B) \
    EX(X,0) EX(X,1) \
    "v_smfmac_f32_32x32x32_bf16 " OREG(UP) ", " V1P ", " RNGO(Y,8,8) ", v23\n\t" \
    MID_A \
    EX(X,2) EX(X,3) EX(X,4) \
    MID_B \
    "v_mfma_f32_32x32x16_bf16 " RNG(Y,16) ", " KN ", " QREG(UN) ", v[24:39]\n\t" \
    EX(X,5) EX(X,6) EX(X,7) EX(X,8) EX(X,9) EX(X,10) EX(X,11) \
    "v_smfmac_f32_32x32x32_bf16 " OREG(U) ", " V0C ", " RNGO(X,0,8) ", v23\n\t" \
    EX(X,12) EX(X,13) EX(X,14) EX(X,15)
#define KE "v[40:43]"
#define V0E "v[44:47]"
#define V1E "v[48:51]"
#define KO "v[52:55]"
#define V0O "v[56:59]"
#define V1O "v[60:63]"
#define LOADS(K, V0, V1) \
    "s_add_u32 %[so], %[so], 2048\n\t" \
    "buffer_load_dwordx4 " K ", v22, %[rs], %[so] offen sc1\n\t" \
    "s_mov_b64 exec, %[msk]\n\t" \
    "buffer_load_dwordx4 " V0 ", v22, %[rs], %[so] offen offset:1024 sc1\n\t" \
    "buffer_load_dwordx4 " V1 ", v22, %[rs], %[so] offen offset:1280 sc1\n\t" \
    "s_mov_b64 exec, -1\n\t"
#define WAITV "s_waitcnt vmcnt(0)\n\t"
#define TILE(K, V0, V1, KP, V0P, V1P) \
    UNIT(64, 80, 0, 7, 1, V1P, V0, K, LOADS(KP, V0P, V1P), "") \
    UNIT(80, 64, 1, 0, 2, V1, V0, K, "", "") \
    UNIT(64, 80, 2, 1, 3, V1, V0, K, "", "") \
    UNIT(80, 64, 3, 2, 4, V1, V0, K, "", "") \
    UNIT(64, 80, 4, 3, 5, V1, V0, K, "", "") \
    UNIT(80, 64, 5, 4, 6, V1, V0, K, "", "") \
    UNIT(64, 80, 6, 5, 7, V1, V0, K, "", "") \
    UNIT(80, 64, 7, 6, 0, V1, V0, KP, "", WAITV)

__device__ __forceinline__ void attn_pass(const u32x32& q, const f32x16& c, u32x4 k0, u32x4 v0, u32x4 v1, unsigned voff, __amdgpu_buffer_rsrc_t rs, unsigned so, unsigned onesv,
                                          f32x32& o01, f32x32& o23, f32x32& o45, f32x32& o67) {
    const u64 msk = 0x0000FFFF0000FFFFull;
    int cnt = 4;
    asm volatile(
        "v_mov_b32 v23, 0xdddddddd\n\ts_nop 1\n\t"
        "v_mfma_f32_32x32x16_bf16 v[64:79], v[40:43], v[96:99], v[24:39]\n\t"
        "v_mov_b32 v56, %[one]\n\tv_mov_b32 v57, %[one]\n\tv_mov_b32 v58, %[one]\n\tv_mov_b32 v59, %[one]\n\t"
        "v_mov_b32 v60, %[one]\n\tv_mov_b32 v61, %[one]\n\tv_mov_b32 v62, %[one]\n\tv_mov_b32 v63, %[one]\n\t"
        ".set i_, 88\n\t.rept 8\n\tv_mov_b32 v[i_], 0\n\t.set i_, i_+1\n\t.endr\n\t"
        "1:\n\t"
        TILE(KE, V0E, V1E, KO, V0O, V1O)
        TILE(KO, V0O, V1O, KE, V0E, V1E)
        "s_sub_u32 %[cnt], %[cnt], 1\n\t"
        "s_cmp_lg_u32 %[cnt], 0\n\t"
        "s_cbranch_scc1 1b\n\t"
        "s_nop 1\n\t"
        "v_smfmac_f32_32x32x32_bf16 " OREG(7) ", " V1O ", v[88:95], v23\n\t"
        "s_nop 7\n\ts_nop 7\n\t"
        : "+{v[128:159]}"(o01), "+{v[160:191]}"(o23), "+{v[192:223]}"(o45), "+{v[224:255]}"(o67), [cnt] "+s"(cnt), [so] "+s"(so),
          "+{v[40:43]}"(k0), "+{v[44:47]}"(v0), "+{v[48:51]}"(v1)
        : "{v[96:127]}"(q), "{v[24:39]}"(c), "{v22}"(voff), [rs] "s"(rs), [msk] "s"(msk), [one] "v"(onesv)
        : "memory", "scc", "v23",
          "v52","v53","v54","v55","v56","v57","v58","v59","v60","v61","v62","v63",
          "v64","v65","v66","v67","v68","v69","v70","v71","v72","v73","v74","v75","v76","v77","v78","v79",
          "v80","v81","v82","v83","v84","v85","v86","v87","v88","v89","v90","v91","v92","v93","v94","v95");
}

constexpr int L_PART = 65536, P2_PART = 1280, P2_M = 8 * 8 * P2_PART;
__device__ __forceinline__ void wo_issue(const char* wo, int ct, unsigned lds_dst, int pc0, int step, int n, int lane) {
    const __amdgpu_buffer_rsrc_t rw = __builtin_amdgcn_make_buffer_rsrc((void*)wo, 0, 512 * 1024, 0x00020000);
    const unsigned vo = (lane >> 3) * 1024 + ct * 128 + (((lane & 7) ^ (((lane >> 4) & 1) << 2)) << 4);
    unsigned sv, t = pc0 * 8192, cnt = n;
    asm volatile("s_mov_b32 %[sv], m0\n\ts_mov_b32 m0, %[dst]\n\ts_nop 0\n\t"
                 "1:\n\tbuffer_load_dwordx4 %[vo], %[rw], %[t] offen lds\n\t"
                 "s_add_u32 m0, m0, %[dm]\n\ts_add_u32 %[t], %[t], %[ds]\n\ts_sub_u32 %[cnt], %[cnt], 1\n\ts_cmp_lg_u32 %[cnt], 0\n\ts_cbranch_scc1 1b\n\t"
                 "s_mov_b32 m0, %[sv]"
                 : [sv] "=&s"(sv), [t] "+s"(t), [cnt] "+s"(cnt) : [vo] "v"(vo), [rw] "s"(rw), [dst] "s"(lds_dst + pc0 * 1024), [dm] "s"(step * 1024), [ds] "s"(step * 8192) : "memory", "scc");
}
__device__ __forceinline__ unsigned wo_lane_off(int lane) {
    const unsigned h = lane >> 5, q4 = (lane & 15) >> 2, p4 = lane & 3;
    return (4 * h + q4) * 128 + (((((lane >> 4) & 1) * 2 + (p4 >> 1)) ^ ((q4 >> 1) << 2)) << 4) + (p4 & 1) * 8;
}

__device__ __forceinline__ void phase2(const Params& p, char* smem, __amdgpu_buffer_rsrc_t rs, u64& tag, bf16x8 (&wfB)[16][2], u32x4 (&ofB)[16], f32x16& accB0, f32x16& accB1) {
    int tid = threadIdx.x; asm volatile("" : "+v"(tid));
    int lane = tid & 63, l31 = lane & 31;
    const int w = __builtin_amdgcn_readfirstlane(tid >> 6);
    const int b = blockIdx.x, j = b & 7, i = b >> 3;
    const int hd = 4 * j + (i & 3), rb = i >> 2;
    f32x32 o01, o23, o45, o67;
    asm volatile(".set i_, 128\n\t.rept 128\n\tv_mov_b32 v[i_], 0\n\t.set i_, i_+1\n\t.endr"
                 : "={v[128:159]}"(o01), "={v[160:191]}"(o23), "={v[192:223]}"(o45), "={v[224:255]}"(o67));
    volatile LAS unsigned* wgw = (volatile LAS unsigned*)((lptr)smem + L_PART + P2_M + 4160);
    if (tid < 8) wgw[tid] = 0;
    if (tid == 0) { wgw[10] = (unsigned)tag; wgw[11] = (unsigned)(tag >> 32); }
    if (w == 0) poll_flags(p.ws + WS_FLAG + FL_KV + b * 128, 16, tag, lane);
    LDS_BAR();
    const unsigned kvoff = WS_KV + hd * 131072 + w * 8 * 2048;
    lptr qst = (lptr)smem + L_PART;
    const u32x4 qw = __builtin_amdgcn_raw_buffer_load_b128(rs, WS_Q + (hd * 64 + 8 * rb + w) * 1024 + lane * 16, 0, SC1);
    const u32x4 q0 = __builtin_amdgcn_raw_buffer_load_b128(rs, WS_Q + (hd * 64 + 8 * rb) * 1024 + lane * 16, 0, SC1);
    const u32x4 kr = __builtin_amdgcn_raw_buffer_load_b128(rs, WS_KV + hd * 131072 + lane * 16, 0, SC1);
    const u32x4 k0 = __builtin_amdgcn_raw_buffer_load_b128(rs, kvoff + lane * 16, 0, SC1);
    u32x4 v0 = __builtin_amdgcn_raw_buffer_load_b128(rs, kvoff + 1024 + lane * 16, 0, SC1);
    u32x4 v1 = __builtin_amdgcn_raw_buffer_load_b128(rs, kvoff + 1280 + lane * 16, 0, SC1);
    tag = ((u64)(unsigned)__builtin_amdgcn_readfirstlane(wgw[11]) << 32) | (unsigned)__builtin_amdgcn_readfirstlane(wgw[10]);
    *(LAS u32x4*)(qst + w * 1024 + lane * 16) = qw;
    float m;
    {
        const f32x16 z = {};
        m = swap32max(max16(MFMA32(__builtin_bit_cast(bf16x8, kr), __builtin_bit_cast(bf16x8, q0), z)));
    }
    asm volatile("s_waitcnt vmcnt(0) lgkmcnt(0)\n\ts_barrier" ::: "memory");
    if (w == 0) {
        const int j1 = 2 * (j >> 1) + ((i >> 3) & 1), r1 = 8 * (j & 1) + (i & 7), rt = 2 * r1 + ((lane >> 3) & 1);
        if (lane < 32) st_flag(p.ws + WS_FLAG + FL_PZ + (rb + 8 * lane) * 256 + hd * 8, tag);
        else if (lane < 48 && i >= 16) st_flag(p.ws + WS_FLAG + FL_OB + ((rt >> 2) + 8 * (8 * (rt & 3) + (lane & 7))) * 128 + j1 * 8, tag);
    }
    u32x32 q;
#pragma unroll
    for (int u = 0; u < 8; ++u) { const u32x4 t = *(const LAS u32x4*)(qst + u * 1024 + lane * 16); q[4 * u] = t[0]; q[4 * u + 1] = t[1]; q[4 * u + 2] = t[2]; q[4 * u + 3] = t[3]; }
    LDS_BAR();
    lptr part = (lptr)smem + L_PART;
    {
        f32x16 c;
#pragma unroll
        for (int g = 0; g < 16; ++g) c[g] = -m;
        const unsigned onesv = (l31 == 16 || l31 == 20) ? 0x3f803f80u : 0u;
        const u32x4 on = {onesv, onesv, onesv, onesv};
        v0 = l31 < 16 ? v0 : on; v1 = l31 < 16 ? v1 : on;
        attn_pass(q, c, k0, v0, v1, lane * 16, rs, kvoff, onesv, o01, o23, o45, o67);
        tid = threadIdx.x; asm volatile("" : "+v"(tid)); lane = tid & 63; l31 = lane & 31;
        lptr pw = part + w * (8 * P2_PART);
#define P2_PUT(U, O, B) do { *(LAS u32x4*)(pw + (U) * P2_PART + lane * 16) = pack8(O[B], O[B + 1], O[B + 2], O[B + 3], O[B + 4], O[B + 5], O[B + 6], O[B + 7]); \
        *(LAS float*)(pw + (U) * P2_PART + 1024 + lane * 4) = O[B + 8]; } while (0)
        P2_PUT(0, o01, 0); P2_PUT(1, o01, 16); P2_PUT(2, o23, 0); P2_PUT(3, o23, 16);
        P2_PUT(4, o45, 0); P2_PUT(5, o45, 16); P2_PUT(6, o67, 0); P2_PUT(7, o67, 16);
#undef P2_PUT
    }
    if (w < 4) {
        const int ct = i & 7;
        wo_issue(p.woB, ct, (unsigned)(uintptr_t)smem, 32 * (w >> 1) + (w & 1), 2, 16, lane);
        poll_flags(p.ws + WS_FLAG + FL_OB + b * 128, 8, tag, lane);
        if (w == 0) poll_flags(p.ws + WS_FLAG + FL_PZ + b * 256, 32, tag, lane);
        asm volatile("s_waitcnt vmcnt(0)" ::: "memory");
        volatile LAS int* pf = (volatile LAS int*)((lptr)smem + L_PART + P2_M + 4160);
        if (lane == 0) pf[w] = 1;
#pragma unroll 1
        for (int spin = 0; spin < (1 << 20) && pf[w ^ 1] == 0; ++spin) __builtin_amdgcn_s_sleep(1);
        asm volatile("" ::: "memory");
        {
            lcptr wb = (lcptr)smem + (w >> 1) * 32768 + wo_lane_off(lane), wc = (lcptr)((unsigned)(uintptr_t)wb ^ 64u);
#pragma unroll
            for (int u = 0; u < 16; ++u) { wfB[u][0] = cat2(trread(wb + u * 2048), trread(wb + u * 2048 + 1024)); wfB[u][1] = cat2(trread(wc + u * 2048), trread(wc + u * 2048 + 1024)); }
        }
        asm volatile("s_waitcnt lgkmcnt(0)" ::: "memory");
        if (lane == 0) pf[4 + w] = 1;
#pragma unroll 1
        for (int spin = 0; spin < (1 << 20) && (pf[4] & pf[5] & pf[6] & pf[7]) == 0; ++spin) __builtin_amdgcn_s_sleep(1);
        asm volatile("" ::: "memory");
        wo_issue(p.woA, ct, (unsigned)(uintptr_t)smem, w, 4, 16, lane);
        asm volatile("s_waitcnt vmcnt(0)" ::: "memory");
    }
#define P2_COMBINE(BAD, DO_STORE) do { \
        u32x4 idn; \
        _Pragma("unroll") for (int e = 0; e < 4; ++e) {        \
            const int d0 = ((2 * e) & 3) + 8 * (e >> 1) + 4 * (lane >> 5); \
            idn[e] = (l31 == d0 ? 0x3f80u : 0u) | (l31 == d0 + 1 ? 0x3f800000u : 0u); } \
        f32x16 oa = {}; float l = 0.f; \
        _Pragma("unroll") for (int v = 0; v < 8; ++v) { \
            lcptr pr = (lcptr)part + v * (8 * P2_PART) + w * P2_PART; \
            const bf16x8 a_ = *(const LAS bf16x8*)(pr + lane * 16); \
            l += *(const LAS float*)(pr + 1024 + lane * 4); \
            oa = MFMA32(__builtin_bit_cast(bf16x8, idn), a_, oa); \
            if (v == 3) __builtin_amdgcn_sched_barrier(0); } \
        float chk = __builtin_fabsf(oa[0]); \
        _Pragma("unroll") for (int e = 1; e < 8; ++e) chk = __builtin_fmaxf(chk, __builtin_fabsf(oa[e])); \
        BAD = __any(!(l >= 1.0e-18f && l <= 1.0e18f && chk <= 3.0e38f)); \
        if (DO_STORE) { const float rl = __builtin_amdgcn_rcpf(l); \
            __builtin_amdgcn_raw_buffer_store_b128(pack8s<0>(oa, rl), rs, WS_O + ((8 * rb + w) * 64 + hd) * 1024 + lane * 16, 0, SC1); } } while (0)
    accB0 = f32x16{}; accB1 = f32x16{};
    LDS_BAR();
    if (w < 4) {
        const int rt64 = 4 * j + (i >> 3);
#pragma unroll
        for (int u = 0; u < 16; ++u) ofB[u] = __builtin_amdgcn_raw_buffer_load_b128(rs, WS_O + ((2 * rt64 + (w & 1)) * 64 + 32 + 16 * (w >> 1) + u) * 1024 + lane * 16, 0, SC1);
    }
    int mybad;
    P2_COMBINE(mybad, !mybad);
    if (lane == 0) *(LAS int*)(part + P2_M + 4096 + w * 4) = mybad;
    LDS_BAR();
    int ab = 0;
#pragma unroll
    for (int v = 0; v < 8; ++v) ab |= *(const LAS int*)(part + P2_M + 4096 + v * 4);
    if (__builtin_expect(__builtin_amdgcn_readfirstlane(ab), 0)) {
        if (w < 4) {
#pragma unroll
            for (int u = 0; u < 16; ++u) { const bf16x8 ob = __builtin_bit_cast(bf16x8, ofB[u]); accB0 = MFMA32(wfB[u][0], ob, accB0); accB1 = MFMA32(wfB[u][1], ob, accB1); }
        }
#pragma unroll
        for (int u = 0; u < 16; ++u) { ofB[u] = (u32x4){0, 0, 0, 0}; wfB[u][0] = __builtin_bit_cast(bf16x8, ofB[u]); wfB[u][1] = wfB[u][0]; }
        lptr mx = part + P2_M + 2048;
#pragma unroll 1
        for (int u = 0; u < 8; ++u) {
            const bf16x8 qu = __builtin_bit_cast(bf16x8, __builtin_amdgcn_raw_buffer_load_b128(rs, WS_Q + (hd * 64 + 8 * rb + u) * 1024 + lane * 16, 0, SC1));
            float mm = -__builtin_inff();
#pragma unroll 1
            for (int t = 0; t < 8; ++t) {
                const bf16x8 kt = __builtin_bit_cast(bf16x8, __builtin_amdgcn_raw_buffer_load_b128(rs, kvoff + t * 2048 + lane * 16, 0, SC1));
                const f32x16 z = {};
                mm = __builtin_fmaxf(mm, max16(MFMA32(kt, qu, z)));
            }
            mm = swap32max(mm);
            __syncthreads();
            *(LAS float*)(mx + w * 256 + lane * 4) = mm;
            __syncthreads();
            float Mu = -__builtin_inff();
#pragma unroll 1
            for (int v = 0; v < 8; ++v) Mu = __builtin_fmaxf(Mu, *(const LAS float*)(mx + v * 256 + lane * 4));
            f32x16 c, oo = {};
#pragma unroll
            for (int g = 0; g < 16; ++g) c[g] = -Mu;
#pragma unroll 1
            for (int t = 0; t < 8; ++t) {
                const bf16x8 kt = __builtin_bit_cast(bf16x8, __builtin_amdgcn_raw_buffer_load_b128(rs, kvoff + t * 2048 + lane * 16, 0, SC1));
                u32x4 v0 = __builtin_amdgcn_raw_buffer_load_b128(rs, kvoff + t * 2048 + 1024 + (lane >> 5) * 512 + (lane & 15) * 16, 0, SC1);
                u32x4 v1 = __builtin_amdgcn_raw_buffer_load_b128(rs, kvoff + t * 2048 + 1280 + (lane >> 5) * 512 + (lane & 15) * 16, 0, SC1);
                const unsigned onesv = (l31 == 16 || l31 == 20) ? 0x3f803f80u : 0u;
                const u32x4 on = {onesv, onesv, onesv, onesv};
                v0 = l31 < 16 ? v0 : on; v1 = l31 < 16 ? v1 : on;
                f32x16 s = MFMA32(kt, qu, c);
#pragma unroll
                for (int g = 0; g < 16; ++g) s[g] = __builtin_amdgcn_exp2f(s[g]);
                oo = SMFMAC(__builtin_bit_cast(bf16x8, v0), lo8(s), oo);
                oo = SMFMAC(__builtin_bit_cast(bf16x8, v1), hi8(s), oo);
            }
            lptr pw = part + w * (8 * P2_PART) + u * P2_PART;
            *(LAS u32x4*)(pw + lane * 16) = pack8(oo[0], oo[1], oo[2], oo[3], oo[4], oo[5], oo[6], oo[7]);
            *(LAS float*)(pw + 1024 + lane * 4) = oo[8];
        }
        LDS_BAR();
        int again;
        P2_COMBINE(again, mybad);
        (void)again;
        LDS_BAR();
    }
#undef P2_COMBINE
    if (tid < 33) st_flag(p.ws + WS_FLAG + (tid < 32 ? FL_OA + (rb + 8 * tid) * 256 + hd * 8 : FL_ALL + b * 8), tag);
}

__device__ __forceinline__ void phase3(const Params& p, char* smem, __amdgpu_buffer_rsrc_t rs, u64 tag, const bf16x8 (&wfB)[16][2], const u32x4 (&ofB)[16], f32x16 accB0, f32x16 accB1) {
    int tid = threadIdx.x; asm volatile("" : "+v"(tid));
    const int lane = tid & 63, l31 = lane & 31, h = lane >> 5;
    const int w = __builtin_amdgcn_readfirstlane(tid >> 6);
    const int b = blockIdx.x, j = b & 7, i = b >> 3;
    const int rt64 = 4 * j + (i >> 3), ct = i & 7;
    const int rt = w & 1, kq = w >> 1;
    if (w < 4) {
#pragma unroll
        for (int u = 0; u < 16; ++u) accB0 = MFMA32(wfB[u][0], __builtin_bit_cast(bf16x8, ofB[u]), accB0);
    }
    asm volatile("" : "+v"(accB0));
    __builtin_amdgcn_sched_barrier(0);
    const float g = __uint_as_float(__builtin_amdgcn_raw_buffer_load_b32(rs, WS_G + (64 * rt64 + 32 * rt + l31) * 4, 0, SC1));
    u32x4 of[8];
#pragma unroll
    for (int u = 0; u < 8; ++u) of[u] = __builtin_amdgcn_raw_buffer_load_b128(rs, WS_O + ((2 * rt64 + rt) * 64 + 8 * kq + u) * 1024 + lane * 16, 0, SC1);
    __builtin_amdgcn_sched_barrier(0);
    if (w < 4) {
#pragma unroll
        for (int u = 0; u < 16; ++u) accB1 = MFMA32(wfB[u][1], __builtin_bit_cast(bf16x8, ofB[u]), accB1);
    }
    asm volatile("" : "+v"(accB1));
    __builtin_amdgcn_sched_barrier(0);
    bf16x8 wf[8][2];
    {
        lcptr wb = (lcptr)smem + kq * 16384 + wo_lane_off(lane), wc = (lcptr)((unsigned)(uintptr_t)wb ^ 64u);
#pragma unroll
        for (int u = 0; u < 8; ++u) { wf[u][0] = cat2(trread(wb + u * 2048), trread(wb + u * 2048 + 1024)); wf[u][1] = cat2(trread(wc + u * 2048), trread(wc + u * 2048 + 1024)); }
    }
#pragma unroll 1
    for (int spin = 0; spin < 4096; ++spin) {
        unsigned mx = 0;
#pragma unroll
        for (int u = 0; u < 8; ++u) mx = max(max(mx, max(of[u][0], of[u][1])), max(of[u][2], of[u][3]));
        if (!__any(mx == ~0u)) break;
        if (spin == 0) poll_flags(p.ws + WS_FLAG + FL_OA + b * 256 + kq * 64, 8, tag, lane);
#pragma unroll
        for (int u = 0; u < 8; ++u) of[u] = __builtin_amdgcn_raw_buffer_load_b128(rs, WS_O + ((2 * rt64 + rt) * 64 + 8 * kq + u) * 1024 + lane * 16, 0, SC1);
    }
    f32x16 a0 = {}, a1 = {};
#pragma unroll
    for (int u = 0; u < 8; ++u) { const bf16x8 ob = __builtin_bit_cast(bf16x8, of[u]); a0 = MFMA32(wf[u][0], ob, a0); a1 = MFMA32(wf[u][1], ob, a1); }
    if (b == 0 && w == 1) {
#pragma unroll 1
        for (int k = 0; k < 4; ++k) poll_flags(p.ws + WS_FLAG + FL_ALL + k * 512, 64, tag, lane);
        if (lane == 0) st_flag(p.ws + WS_FLAG + FL_EPOCH, tag);
    }
    {
        lptr pb = (lptr)smem + L_PART + kq * (64 * 272) + (32 * rt + l31) * 272 + h * 16;
        const float gb = 1.f - g;
#pragma unroll
        for (int g4 = 0; g4 < 4; ++g4) {
            f32x4 v0, v1;
#pragma unroll
            for (int c = 0; c < 4; ++c) { v0[c] = g * a0[4 * g4 + c] + gb * accB0[4 * g4 + c]; v1[c] = g * a1[4 * g4 + c] + gb * accB1[4 * g4 + c]; }
            *(LAS f32x4*)(pb + g4 * 32) = v0;
            *(LAS f32x4*)(pb + 128 + g4 * 32) = v1;
        }
    }
    LDS_BAR();
    {
        lcptr pb = (lcptr)smem + L_PART + (tid >> 3) * 272 + (tid & 7) * 32;
        float v[8];
#pragma unroll
        for (int e = 0; e < 2; ++e) {
            const f32x4 A0 = *(const LAS f32x4*)(pb + e * 16), A1 = *(const LAS f32x4*)(pb + 64 * 272 + e * 16);
            const f32x4 B0 = *(const LAS f32x4*)(pb + 2 * 64 * 272 + e * 16), B1 = *(const LAS f32x4*)(pb + 3 * 64 * 272 + e * 16);
#pragma unroll
            for (int c = 0; c < 4; ++c) v[4 * e + c] = (A0[c] + A1[c]) + (B0[c] + B1[c]);
        }
        *(u32x4*)(p.out + (size_t)(64 * rt64 + (tid >> 3)) * 1024 + ct * 128 + (tid & 7) * 16) = pack8(v[0], v[1], v[2], v[3], v[4], v[5], v[6], v[7]);
    }
}

__global__ __launch_bounds__(512, 2) void fused_kernel(Params p) {
    extern __shared__ __attribute__((aligned(16))) char smem[];
    const __amdgpu_buffer_rsrc_t rs = __builtin_amdgcn_make_buffer_rsrc(p.ws, 0, WS_END, 0x00020000);
    u64 tag = 0;
    phase1(p, smem, rs, tag);
    bf16x8 wfB[16][2]; u32x4 ofB[16]; f32x16 accB0, accB1;
    phase2(p, smem, rs, tag, wfB, ofB, accB0, accB1);
    phase3(p, smem, rs, tag, wfB, ofB, accB0, accB1);
}

extern "C" void kernel_launch(void* const* d_in, const int* in_sizes, int n_in, void* d_out, int out_size, void* d_ws, size_t ws_size,
                              hipStream_t stream) {
    Params p;
    p.x = (const char*)d_in[0]; p.wA = (const char*)d_in[1]; p.woA = (const char*)d_in[2];
    p.wB = (const char*)d_in[4]; p.woB = (const char*)d_in[5]; p.gw = (const char*)d_in[6];
    p.out = (char*)d_out; p.ws = (char*)d_ws;
    (void)hipFuncSetAttribute((const void*)fused_kernel, hipFuncAttributeMaxDynamicSharedMemorySize, LDS_BYTES);
    fused_kernel<<<256, 512, LDS_BYTES, stream>>>(p);
}
```
